```python
import torch

def _softmax(x, axis=-1):
    return torch.softmax(x, dim=axis)
def _gelu(x):
    return torch.nn.functional.gelu(x, approximate="tanh")
def _silu(x):
    return torch.nn.functional.silu(x)
def _sigmoid(x):
    return torch.sigmoid(x)
def _layernorm(x, axis=-1, eps=1e-5):
    m = x.mean(dim=axis, keepdim=True)
    v = x.var(dim=axis, keepdim=True, unbiased=False)
    return (x - m) / torch.sqrt(v + eps)
def _scatter_add(tgt, idx, src, axis=0):
    return tgt.clone().index_add_(axis, idx, src)
def _segment_sum(src, seg, n):
    out = torch.zeros((n,) + src.shape[1:], dtype=src.dtype)
    return out.index_add_(0, seg, src)
def _pad(x, pw):
    flat = tuple(v for pair in reversed(pw) for v in pair)
    return torch.nn.functional.pad(x, flat)

kernel_name = 'gated_mix__softmax_full_mha__dense_activation_squared_relu__prompt'

def setup_inputs():

    _w = (lambda a: (lambda t: t.to(torch.bfloat16) if t.is_floating_point() else t)(torch.as_tensor(a)))
    def _A_setup():
        B, S, D = 1, 2048, 512
        return {
            "x": _w((lambda *s: torch.randn(*s, dtype=torch.bfloat16) * 0.05)(B, S, D)),
            "wqkv": _w((lambda *s: torch.randn(*s, dtype=torch.bfloat16) * 0.8)(D, 3 * D)),
            "wo": _w((lambda *s: torch.randn(*s, dtype=torch.bfloat16) * 0.1)(D, D)),
        }
    _A_in = _A_setup()

    def _B_setup():
        B, S, D, F = 1, 2048, 512, 1792
        return {
            "x": _w((lambda *s: torch.randn(*s, dtype=torch.bfloat16) * 0.05)(B, S, D)),
            "w1": _w((lambda *s: torch.randn(*s, dtype=torch.bfloat16) * 0.1)(D, F)),
            "b1": _w((lambda *s: torch.randn(*s, dtype=torch.bfloat16) * 0.05)(F)),
            "w2": _w((lambda *s: torch.randn(*s, dtype=torch.bfloat16) * 0.05)(F, D)),
            "b2": _w((lambda *s: torch.randn(*s, dtype=torch.bfloat16) * 0.05)(D)),
        }
    _B_in = _B_setup()

    return {
        "A_x": _A_in["x"],
        "A_wqkv": _A_in["wqkv"],
        "A_wo": _A_in["wo"],
        "B_x": _B_in["x"],
        "B_w1": _B_in["w1"],
        "B_b1": _B_in["b1"],
        "B_w2": _B_in["w2"],
        "B_b2": _B_in["b2"],
        "gate_w": _w((lambda *s: torch.randn(*s, dtype=torch.bfloat16) * 0.05)(512) * 16.0),
    }

def reference(A_x, A_wqkv, A_wo, B_x, B_w1, B_b1, B_w2, B_b2, gate_w):
    def _A_ref(x, wqkv, wo):
        B, S, D = x.shape
        H = 32; Dh = D // H
        qkv = (lambda x, *d: x.permute(*d))((x @ wqkv).reshape(B, S, 3, H, Dh), 2, 0, 3, 1, 4)
        q, k, v = qkv[0], qkv[1], qkv[2]
        scale = Dh ** -0.5
        scores = torch.einsum("bhqd,bhkd->bhqk", q, k) * scale
        p = _softmax(scores, axis=-1)
        o = torch.einsum("bhqk,bhkd->bhqd", p, v)
        return (lambda x, *d: x.permute(*d))(o, 0, 2, 1, 3).reshape(B, S, D) @ wo

    def _B_ref(x, w1, b1, w2, b2):
        h = torch.einsum("bsd,df->bsf", x, w1) + b1
        zero = torch.zeros_like(h)
        r = torch.where(h > zero, h, zero)
        h = r * r
        out = torch.einsum("bsf,fd->bsd", h, w2) + b2
        return out

    x = A_x
    g = _sigmoid((x * gate_w).sum(-1, keepdims=True))
    a = _A_ref(x, A_wqkv, A_wo)
    b = _B_ref(x, B_w1, B_b1, B_w2, B_b2)
    return g * a + (1 - g) * b
```

```cpp
#include <hip/hip_runtime.h>
#include <hip/hip_bf16.h>

namespace {

typedef __attribute__((ext_vector_type(8))) short bf16x8;
typedef __attribute__((ext_vector_type(4))) short s16x4;
typedef __attribute__((ext_vector_type(16))) float f32x16;
typedef __attribute__((ext_vector_type(4))) float f32x4;
typedef __attribute__((ext_vector_type(4))) unsigned u32x4;
typedef __attribute__((ext_vector_type(2))) unsigned u32x2;
typedef __attribute__((ext_vector_type(2))) __bf16 bf2_t;
typedef __attribute__((ext_vector_type(2))) float f2_t;
typedef unsigned short u16;

#define DI __device__ __forceinline__
#define MFMA32(a, b, c) __builtin_amdgcn_mfma_f32_32x32x16_bf16((a), (b), (c), 0, 0, 0)

constexpr int SEQ = 2048, DM = 512, FF = 1792, NH = 32;
constexpr float QSCALE = 0.25f * 1.4426950408889634f;
constexpr float RESCALE_THR = 4.0f;

constexpr size_t WS_QF = 0;
constexpr size_t WS_KF = WS_QF + (size_t)NH * 64 * 1024;
constexpr size_t WS_VF = WS_KF + (size_t)NH * 64 * 1024;
constexpr size_t WS_HF = WS_VF + (size_t)NH * 64 * 1024;
constexpr size_t WS_W2F = WS_HF + (size_t)64 * 112 * 1024;
constexpr size_t WS_WOF = WS_W2F + (size_t)16 * 112 * 1024;
constexpr size_t WS_OF = WS_WOF + (size_t)16 * 32 * 1024;
constexpr size_t WS_BB = WS_OF + (size_t)64 * 32 * 1024;
constexpr size_t WS_GATE = WS_BB + (size_t)64 * 16 * 2048;
constexpr size_t WS_END = WS_GATE + (size_t)SEQ * 4;

DI unsigned pk_bf16(float a, float b) {
  bf2_t r = __builtin_convertvector((f2_t){a, b}, bf2_t);
  return __builtin_bit_cast(unsigned, r);
}
DI float bf16lo(unsigned p) { return __builtin_bit_cast(float, p << 16); }
DI float bf16hi(unsigned p) { return __builtin_bit_cast(float, p & 0xffff0000u); }
DI float bf2f(u16 v) { return __builtin_bit_cast(float, (unsigned)v << 16); }
DI float rbf(float x) { return bf16lo(pk_bf16(x, 0.f)); }

DI u32x4 swap_to_frag(unsigned p0, unsigned p1, unsigned p2, unsigned p3) {
  auto r0 = __builtin_amdgcn_permlane32_swap(p0, p2, false, false);
  auto r1 = __builtin_amdgcn_permlane32_swap(p1, p3, false, false);
  u32x4 o;
  o[0] = r0[0]; o[1] = r1[0]; o[2] = r0[1]; o[3] = r1[1];
  return o;
}

DI void glds16(const void* g, void* l) {
  __builtin_amdgcn_global_load_lds((const __attribute__((address_space(1))) void*)(g),
                                   (__attribute__((address_space(3))) void*)(l), 16, 0, 0);
}
#define STAMP(ws, slot)
#define STAMP2(ws, slot)
#define WAIT_VM(n) asm volatile("s_waitcnt vmcnt(" #n ")" ::: "memory")
#define BARRIER() __builtin_amdgcn_s_barrier()

constexpr int PJ_PITCH = 448;
constexpr int PJ_WCH = 64 * PJ_PITCH;
constexpr int PJ_XCH = 128 * 128;
constexpr int PJ_STAGE = PJ_WCH + PJ_XCH;
constexpr int PJ_LDS = 3 * PJ_STAGE;

template <bool QKV>
DI void proj_body(char* lds, const u16* __restrict__ x, const u16* __restrict__ W, const u16* __restrict__ b1,
                  char* ws, int mt, int c, int w, int lane) {
  constexpr int U = QKV ? 6 : 7;
  constexpr int LDW = QKV ? 3 * DM : FF;
  const int h = lane >> 5, r32 = lane & 31;
  const int row0 = mt * 128 + w * 32;
  STAMP(ws, 0);

  f32x16 acc[U];
#pragma unroll
  for (int u = 0; u < U; ++u)
#pragma unroll
    for (int i = 0; i < 16; ++i) acc[u][i] = 0.f;

  int soff[7];
#pragma unroll
  for (int t = 0; t < 7; ++t) {
    const int i = (4 * t + w) * 64 + lane;
    const int r = i / 28, cc = i - 28 * r;
    int col;
    if constexpr (QKV) {
      const int part = cc >> 3, wi = cc & 7;
      col = (part < 3 ? part * DM : 0) + 64 * c + 8 * wi;
    } else {
      col = 224 * (c - 8) + 8 * cc;
    }
    soff[t] = r * LDW + col;
  }
  const u16* xsrc = x + (size_t)(row0 + (lane >> 3)) * DM;
  const int xc0 = 8 * ((lane & 7) ^ ((lane >> 4) & 7)), xc1 = 8 * ((lane & 7) ^ ((4 + (lane >> 4)) & 7));
  auto issue = [&](int kc) {
    char* st = lds + (kc % 3) * PJ_STAGE;
    char* xd = st + PJ_WCH + w * 4096 + lane * 16;
#pragma unroll
    for (int t = 0; t < 4; ++t) glds16(xsrc + (size_t)(8 * t) * DM + 64 * kc + ((t & 1) ? xc1 : xc0), xd + t * 1024);
    const u16* src = W + (size_t)kc * 64 * LDW;
    char* dst = st + (w * 64 + lane) * 16;
#pragma unroll
    for (int t = 0; t < 7; ++t) glds16(src + soff[t], dst + t * 4096);
  };
  issue(0); issue(1); issue(2);

  const int q4 = (lane & 15) >> 2, p4 = lane & 3, blk = (lane >> 4) & 1;
  const int laneoff = (32 * h + q4) * PJ_PITCH + 32 * blk + 8 * p4;
  const int xoff = PJ_WCH + (w * 32 + r32) * 128;
  const int xsw = (r32 >> 1) & 7;

  struct Frags { bf16x8 wf[U]; bf16x8 xf; };
  auto rdw = [&](int kc, int ks, Frags& f) {
    const char* st = lds + (kc % 3) * PJ_STAGE;
    f.xf = *reinterpret_cast<const bf16x8*>(st + xoff + 16 * ((4 * h + ks) ^ xsw));
    const char* base = st + laneoff + ks * 8 * PJ_PITCH;
#pragma unroll
    for (int u = 0; u < U; ++u) {
      const char* pa = base + 64 * u;
      const s16x4 lo = __builtin_amdgcn_ds_read_tr16_b64_v4i16((__attribute__((address_space(3))) s16x4*)(pa));
      const s16x4 hi =
          __builtin_amdgcn_ds_read_tr16_b64_v4i16((__attribute__((address_space(3))) s16x4*)(pa + 4 * PJ_PITCH));
      f.wf[u] = __builtin_shufflevector(lo, hi, 0, 1, 2, 3, 4, 5, 6, 7);
    }
  };
  auto mma = [&](const Frags& f) {
#pragma unroll
    for (int u = 0; u < U; ++u) {
      if (QKV && u >= 4) acc[u] = MFMA32(f.xf, f.wf[u], acc[u]);
      else acc[u] = MFMA32(f.wf[u], f.xf, acc[u]);
    }
  };
  Frags fA, fB;
  WAIT_VM(22);
  BARRIER();
  STAMP(ws, 1);
  rdw(0, 0, fA);
#pragma unroll
  for (int kc = 0; kc < 8; ++kc) {
    rdw(kc, 1, fB);
    mma(fA);
    rdw(kc, 2, fA);
    mma(fB);
    rdw(kc, 3, fB);
    mma(fA);
    if (kc < 7) {
      if (kc <= 5) WAIT_VM(11); else WAIT_VM(0);
      asm volatile("s_waitcnt lgkmcnt(0)" ::: "memory");
      BARRIER();
      if (kc + 3 < 8) issue(kc + 3);
      rdw(kc + 1, 0, fA);
    }
    mma(fB);
  }

  STAMP(ws, 2);
  const int mtile = row0 >> 5;
  if constexpr (QKV) {
    u16* qf = reinterpret_cast<u16*>(ws + WS_QF);
    u16* kf = reinterpret_cast<u16*>(ws + WS_KF);
    u16* vf = reinterpret_cast<u16*>(ws + WS_VF);
#pragma unroll
    for (int u = 0; u < 2; ++u) {
#pragma unroll
      for (int hh = 0; hh < 2; ++hh) {
        const int head = 4 * c + 2 * u + hh;
        {
          const f32x16& a = acc[u];
          const int o = 8 * hh;
          const u32x4 fr = swap_to_frag(pk_bf16(a[o + 0] * QSCALE, a[o + 1] * QSCALE),
                                        pk_bf16(a[o + 2] * QSCALE, a[o + 3] * QSCALE),
                                        pk_bf16(a[o + 4] * QSCALE, a[o + 5] * QSCALE),
                                        pk_bf16(a[o + 6] * QSCALE, a[o + 7] * QSCALE));
          *reinterpret_cast<u32x4*>(qf + ((size_t)(head * 64 + mtile) * 64 + lane) * 8) = fr;
        }
        {
          const f32x16& a = acc[2 + u];
          const int o = 8 * hh;
          const unsigned p0 = pk_bf16(a[o + 0], a[o + 1]), p1 = pk_bf16(a[o + 2], a[o + 3]);
          const unsigned p2 = pk_bf16(a[o + 4], a[o + 5]), p3 = pk_bf16(a[o + 6], a[o + 7]);
          const u32x4 fr = swap_to_frag(p0, p1, p2, p3);
          *reinterpret_cast<u32x4*>(kf + ((size_t)(head * 64 + mtile) * 64 + lane) * 8) = fr;
        }
      }
      {
        const f32x16& a = acc[4 + u];
        const int head = 4 * c + 2 * u + (r32 >> 4), d = r32 & 15;
#pragma unroll
        for (int s = 0; s < 2; ++s) {
          u32x4 fr;
#pragma unroll
          for (int j = 0; j < 4; ++j) fr[j] = pk_bf16(a[8 * s + 2 * j], a[8 * s + 2 * j + 1]);
          *reinterpret_cast<u32x4*>(vf + ((((size_t)head * 64 + mtile) * 2 + s) * 32 + h * 16 + d) * 8) = fr;
        }
      }
    }
  } else {
    u16* hf = reinterpret_cast<u16*>(ws + WS_HF);
    s16x4 bias[U][4];
#pragma unroll
    for (int u = 0; u < U; ++u)
#pragma unroll
      for (int g = 0; g < 4; ++g)
        bias[u][g] = *reinterpret_cast<const s16x4*>(b1 + 224 * (c - 8) + 32 * u + 4 * h + 8 * g);
#pragma unroll
    for (int u = 0; u < U; ++u) {
      const int f0 = 224 * (c - 8) + 32 * u;
      unsigned pp[8];
#pragma unroll
      for (int j = 0; j < 8; ++j) {
        const unsigned t0 = pk_bf16(acc[u][2 * j], acc[u][2 * j + 1]);
        const unsigned t = pk_bf16(bf16lo(t0) + bf2f((u16)bias[u][j >> 1][2 * (j & 1)]),
                                   bf16hi(t0) + bf2f((u16)bias[u][j >> 1][2 * (j & 1) + 1]));
        const float r0 = fmaxf(bf16lo(t), 0.f), r1 = fmaxf(bf16hi(t), 0.f);
        pp[j] = pk_bf16(r0 * r0, r1 * r1);
      }
#pragma unroll
      for (int s = 0; s < 2; ++s) {
        const u32x4 fr = swap_to_frag(pp[4 * s + 0], pp[4 * s + 1], pp[4 * s + 2], pp[4 * s + 3]);
        const int kstep = (f0 >> 4) + s;
        *reinterpret_cast<u32x4*>(hf + ((size_t)(mtile * 112 + kstep) * 64 + lane) * 8) = fr;
      }
    }
  }
}

constexpr int PJ_SCRATCH = 5 * 1024;

DI u32x4 retile_read(const char* blk, int lane) {
  const int h = lane >> 5, b16 = (lane >> 4) & 1, q4 = (lane & 15) >> 2, p4 = lane & 3;
  const char* pa = blk + (8 * h + q4) * 64 + 32 * b16 + 8 * p4;
  const s16x4 lo = __builtin_amdgcn_ds_read_tr16_b64_v4i16((__attribute__((address_space(3))) s16x4*)(pa));
  const s16x4 hi = __builtin_amdgcn_ds_read_tr16_b64_v4i16((__attribute__((address_space(3))) s16x4*)(pa + 256));
  return __builtin_bit_cast(u32x4, __builtin_shufflevector(lo, hi, 0, 1, 2, 3, 4, 5, 6, 7));
}

__global__ __launch_bounds__(256, 1) void k_proj(const u16* __restrict__ x, const u16* __restrict__ wqkv,
                                                 const u16* __restrict__ w1, const u16* __restrict__ b1,
                                                 const u16* __restrict__ w2, const u16* __restrict__ wo,
                                                 const u16* __restrict__ gate_w, char* ws) {
  __shared__ __attribute__((aligned(16))) char lds[PJ_LDS + 4 * PJ_SCRATCH];
  const int tid = threadIdx.x, lane = tid & 63, w = tid >> 6;
  const int b = blockIdx.x, xcd = b & 7, idx = b >> 3, mt = idx & 15, c = xcd + 8 * (idx >> 4);
  if (c < 8) {
    proj_body<true>(lds, x, wqkv, b1, ws, mt, c, w, lane);
    STAMP(ws, 3);
    const int wv = (mt * 8 + c) * 4 + w;
    u16* w2f = reinterpret_cast<u16*>(ws + WS_W2F);
    u16* wof = reinterpret_cast<u16*>(ws + WS_WOF);
    char* scr = lds + PJ_LDS + w * PJ_SCRATCH;
    const int rr = lane >> 2, cc = lane & 3;
    const bool do_w2 = wv < 448;
    bf16x8 blk[5];
#pragma unroll
    for (int i = 0; i < 4; ++i) {
      const int f = 4 * wv + i, ntile = f / 112, kstep = f - 112 * ntile;
      if (do_w2) blk[i] = *reinterpret_cast<const bf16x8*>(w2 + (size_t)(16 * kstep + rr) * DM + 32 * ntile + 8 * cc);
    }
    {
      const int ntile = wv >> 5, kstep = wv & 31;
      blk[4] = *reinterpret_cast<const bf16x8*>(wo + (size_t)(16 * kstep + rr) * DM + 32 * ntile + 8 * cc);
    }
    const bf16x8 gw = *reinterpret_cast<const bf16x8*>(gate_w + 8 * lane);
    bf16x8 xv[4];
#pragma unroll
    for (int r = 0; r < 4; ++r)
      xv[r] = *reinterpret_cast<const bf16x8*>(x + (size_t)(mt * 128 + 16 * c + 4 * w + r) * DM + 8 * lane);
#pragma unroll
    for (int i = 0; i < 5; ++i)
      if (i == 4 || do_w2) *reinterpret_cast<bf16x8*>(scr + i * 1024 + lane * 16) = blk[i];
#pragma unroll
    for (int i = 0; i < 5; ++i) {
      if (i == 4 || do_w2) {
        const u32x4 fr = retile_read(scr + i * 1024, lane);
        u16* dst = (i == 4) ? (wof + (size_t)wv * 512) : (w2f + (size_t)(4 * wv + i) * 512);
        *reinterpret_cast<u32x4*>(dst + lane * 8) = fr;
      }
    }
    float* gate = reinterpret_cast<float*>(ws + WS_GATE);
#pragma unroll
    for (int r = 0; r < 4; ++r) {
      float sum = 0.f;
#pragma unroll
      for (int j = 0; j < 8; ++j) sum += rbf(bf2f((u16)xv[r][j]) * bf2f((u16)gw[j]));
#pragma unroll
      for (int o = 32; o >= 1; o >>= 1) sum += __shfl_xor(sum, o);
      if (lane == 0) {
        const float z = rbf(sum);
        gate[mt * 128 + 16 * c + 4 * w + r] = rbf(1.f / (1.f + __expf(-z)));
      }
    }
    STAMP(ws, 4);
  } else {
    proj_body<false>(lds, x, w1, b1, ws, mt, c, w, lane);
    STAMP(ws, 3);
    STAMP(ws, 4);
  }
}

constexpr int AT_K = 0, AT_V = 65536, AT_ONES = 131072;
constexpr int AT_LDS = AT_ONES + 8192;

DI bf16x8 lds_b128(const char* p) { return *reinterpret_cast<const bf16x8*>(p); }

DI void soft_pv(const f32x16& S, const bf16x8 v0, const bf16x8 v1, f32x16& O) {
  u32x4 p0, p1;
#pragma unroll
  for (int j = 0; j < 4; ++j) {
    p0[j] = pk_bf16(__builtin_amdgcn_exp2f(S[2 * j]), __builtin_amdgcn_exp2f(S[2 * j + 1]));
    p1[j] = pk_bf16(__builtin_amdgcn_exp2f(S[8 + 2 * j]), __builtin_amdgcn_exp2f(S[8 + 2 * j + 1]));
  }
  O = MFMA32(v0, __builtin_bit_cast(bf16x8, p0), O);
  O = MFMA32(v1, __builtin_bit_cast(bf16x8, p1), O);
}

DI float lane_max16(const f32x16& S) {
  float m = fmaxf(S[0], S[1]);
#pragma unroll
  for (int i = 2; i < 16; i += 2) m = __builtin_fmaxf(fmaxf(m, S[i]), S[i + 1]);
  return m;
}

DI void ffn2_role(const char* __restrict__ ws_in, char* ws_out, const u16* __restrict__ b2, int b, int w2,
                  int lane) {
  const int mt64 = (b & 7) * 4 + ((b >> 3) & 3), nt64 = b >> 5;
  const int mtile = 2 * mt64 + (w2 & 1), ntile = 2 * nt64 + (w2 >> 1), h = lane >> 5;
  const u16* hf = reinterpret_cast<const u16*>(ws_in + WS_HF) + (size_t)mtile * 112 * 512 + lane * 8;
  const u16* wf = reinterpret_cast<const u16*>(ws_in + WS_W2F) + (size_t)ntile * 112 * 512 + lane * 8;
  constexpr int PF = 8;
  bf16x8 ra[PF], rb[PF];
#pragma unroll
  for (int i = 0; i < PF; ++i) {
    ra[i] = *reinterpret_cast<const bf16x8*>(hf + (size_t)i * 512);
    rb[i] = *reinterpret_cast<const bf16x8*>(wf + (size_t)i * 512);
  }
  BARRIER();
  BARRIER();
  f32x16 acc = {};
  for (int k0 = 0; k0 < 112; k0 += PF) {
#pragma unroll
    for (int i = 0; i < PF; ++i) {
      acc = MFMA32(rb[i], ra[i], acc);
      const int kn = k0 + PF + i;
      if (kn < 112) {
        ra[i] = *reinterpret_cast<const bf16x8*>(hf + (size_t)kn * 512);
        rb[i] = *reinterpret_cast<const bf16x8*>(wf + (size_t)kn * 512);
      }
    }
  }
  unsigned pp[8];
#pragma unroll
  for (int gq = 0; gq < 4; ++gq) {
    const s16x4 bv = *reinterpret_cast<const s16x4*>(b2 + ntile * 32 + 8 * gq + 4 * h);
    pp[2 * gq] = pk_bf16(rbf(acc[4 * gq + 0]) + bf2f((u16)bv[0]), rbf(acc[4 * gq + 1]) + bf2f((u16)bv[1]));
    pp[2 * gq + 1] = pk_bf16(rbf(acc[4 * gq + 2]) + bf2f((u16)bv[2]), rbf(acc[4 * gq + 3]) + bf2f((u16)bv[3]));
  }
  u32x4* dst = reinterpret_cast<u32x4*>(ws_out + WS_BB) + ((size_t)(mtile * 16 + ntile) * 64 + lane) * 2;
  dst[0] = (u32x4){pp[0], pp[1], pp[2], pp[3]};
  dst[1] = (u32x4){pp[4], pp[5], pp[6], pp[7]};
}

__global__ __launch_bounds__(768, 3) void k_attn(const char* __restrict__ ws_in, char* ws_out,
                                                 const u16* __restrict__ b2) {
  __shared__ __attribute__((aligned(16))) char lds[AT_LDS];
  const int tid = threadIdx.x, lane = tid & 63, w = tid >> 6, h = lane >> 5, r32 = lane & 31;
  const int b = blockIdx.x;
  if (w >= 8) {
    ffn2_role(ws_in, ws_out, b2, b, w - 8, lane);
    return;
  }
  const int head = 4 * (b & 7) + ((b >> 3) & 3), qb = b >> 5;
  const int qtile = qb * 8 + w;
  const u16* qf = reinterpret_cast<const u16*>(ws_in + WS_QF);
  const u16* kf = reinterpret_cast<const u16*>(ws_in + WS_KF);
  const u16* vf = reinterpret_cast<const u16*>(ws_in + WS_VF);

  STAMP2(ws_out, 0);
  bf16x8 qv;
  {
    const u16* qp = qf + ((size_t)(head * 64 + qtile) * 64 + lane) * 8;
    asm volatile("global_load_dwordx4 %0, %1, off" : "=v"(qv) : "v"(qp) : "memory");
  }
  {
    const u16* ks = kf + (size_t)head * 64 * 512 + lane * 8;
    const u16* vs = vf + (size_t)head * 64 * 512 + lane * 8;
#pragma unroll
    for (int s = 0; s < 8; ++s) {
      const int ch = 8 * s + w;
      glds16(ks + (size_t)ch * 512, lds + AT_K + ch * 1024 + lane * 16);
      glds16(vs + (size_t)ch * 512, lds + AT_V + ch * 1024 + lane * 16);
    }
  }
  {
    u32x4 ones = {0x3f803f80u, 0x3f803f80u, 0x3f803f80u, 0x3f803f80u};
    *reinterpret_cast<u32x4*>(lds + AT_ONES + tid * 16) = ones;
  }
  asm volatile("s_waitcnt vmcnt(13)" : "+v"(qv)::"memory");
  asm volatile("s_waitcnt lgkmcnt(0)" ::: "memory");
  BARRIER();
  STAMP2(ws_out, 1);
  const char* kbase = lds + AT_K + lane * 16;
  const char* vbase = (r32 < 16) ? (lds + AT_V + (h * 16 + r32) * 16) : (lds + AT_ONES);
  const int vstep = (r32 < 16) ? 8192 : 0;

  float m = 0.f;
  f32x16 negm = {};
  f32x16 O = {};
  for (int g = 0; g < 8; ++g) {
    if (g == 1) { STAMP2(ws_out, 2); WAIT_VM(0); BARRIER(); STAMP2(ws_out, 3); }
    const char* kb = kbase + g * 8192;
    const char* vb = vbase + g * vstep;
#pragma unroll
    for (int u = 0; u < 8; ++u) {
      const int c = 8 * g + u;
      f32x16 S = MFMA32(lds_b128(kb + u * 1024), qv, negm);
      const bf16x8 v0 = lds_b128(vb + u * 1024), v1 = lds_b128(vb + u * 1024 + 512);
      const float mx = lane_max16(S);
      if (c == 0 || __any(mx > RESCALE_THR)) {
        const float mnew = m + fmaxf(mx, __shfl_xor(mx, 32));
        if (c > 0) {
          const float alpha = __builtin_amdgcn_exp2f(m - mnew);
#pragma unroll
          for (int i = 0; i < 16; ++i) O[i] *= alpha;
        }
        const float d = mnew - m;
#pragma unroll
        for (int i = 0; i < 16; ++i) { S[i] -= d; negm[i] = -mnew; }
        m = mnew;
      }
      soft_pv(S, v0, v1, O);
    }
  }
  STAMP2(ws_out, 4);
  {
    const float inv = 1.f / O[8];
    const u32x4 fr = swap_to_frag(pk_bf16(O[0] * inv, O[1] * inv), pk_bf16(O[2] * inv, O[3] * inv),
                                  pk_bf16(O[4] * inv, O[5] * inv), pk_bf16(O[6] * inv, O[7] * inv));
    u16* of = reinterpret_cast<u16*>(ws_out + WS_OF);
    *reinterpret_cast<u32x4*>(of + ((size_t)(qtile * 32 + head) * 64 + lane) * 8) = fr;
  }
  STAMP2(ws_out, 5);
}

__global__ __launch_bounds__(256, 1) void k_out(const char* __restrict__ ws, u16* __restrict__ out) {
  __shared__ __attribute__((aligned(16))) char lds[4 * 32 * 1024];
  const int tid = threadIdx.x, lane = tid & 63, w = tid >> 6, h = lane >> 5;
  const int b = blockIdx.x;
  const int mt64 = (b & 7) * 4 + ((b >> 3) & 3), nt64 = b >> 5;
  const int mtile = 2 * mt64 + (w & 1), ntile = 2 * nt64 + (w >> 1);
  {
    const char* src = (w < 2) ? (ws + WS_OF + (size_t)(2 * mt64 + w) * 32 * 1024)
                              : (ws + WS_WOF + (size_t)(2 * nt64 + (w - 2)) * 32 * 1024);
#pragma unroll
    for (int ks = 0; ks < 32; ++ks) glds16(src + ks * 1024 + lane * 16, lds + w * 32768 + ks * 1024 + lane * 16);
  }
  const u32x4* bsrc =
      reinterpret_cast<const u32x4*>(ws + WS_BB) + ((size_t)(mtile * 16 + ntile) * 64 + lane) * 2;
  const int m = mtile * 32 + (lane & 31);
  u32x4 b0, b1v;
  float g;
  {
    const float* gp = reinterpret_cast<const float*>(ws + WS_GATE) + m;
    asm volatile("global_load_dwordx4 %0, %1, off" : "=v"(b0) : "v"(bsrc) : "memory");
    asm volatile("global_load_dwordx4 %0, %1, off offset:16" : "=v"(b1v) : "v"(bsrc) : "memory");
    asm volatile("global_load_dword %0, %1, off" : "=v"(g) : "v"(gp) : "memory");
  }
  const char* ab = lds + (w & 1) * 32768 + lane * 16;
  const char* bb = lds + (2 + (w >> 1)) * 32768 + lane * 16;
  f32x16 aa = {};
#pragma unroll
  for (int st = 0; st < 4; ++st) {
    if (st == 0) WAIT_VM(27); else if (st == 1) WAIT_VM(19); else if (st == 2) WAIT_VM(11);
    else asm volatile("s_waitcnt vmcnt(0)" : "+v"(b0), "+v"(b1v), "+v"(g)::"memory");
    BARRIER();
#pragma unroll
    for (int k = 0; k < 8; ++k) {
      const int ks = 8 * st + k;
      aa = MFMA32(*reinterpret_cast<const bf16x8*>(bb + ks * 1024), *reinterpret_cast<const bf16x8*>(ab + ks * 1024),
                  aa);
    }
  }
  const float omg = rbf(1.f - g);
  unsigned pp[8];
#pragma unroll
  for (int j = 0; j < 8; ++j) {
    const unsigned bp = j < 4 ? b0[j] : b1v[j - 4];
    const float o0 = rbf(g * rbf(aa[2 * j])) + rbf(omg * bf16lo(bp));
    const float o1 = rbf(g * rbf(aa[2 * j + 1])) + rbf(omg * bf16hi(bp));
    pp[j] = pk_bf16(o0, o1);
  }
#pragma unroll
  for (int s = 0; s < 2; ++s) {
    const u32x4 fr = swap_to_frag(pp[4 * s + 0], pp[4 * s + 1], pp[4 * s + 2], pp[4 * s + 3]);
    *reinterpret_cast<u32x4*>(out + (size_t)m * DM + ntile * 32 + 16 * s + 8 * h) = fr;
  }
}

}

extern "C" void kernel_launch(void* const* d_in, const int* in_sizes, int n_in, void* d_out, int out_size,
                              void* d_ws, size_t ws_size, hipStream_t stream) {
  (void)in_sizes; (void)n_in; (void)out_size;
  if (ws_size < WS_END) return;
  const u16* x = (const u16*)d_in[0];
  const u16* wqkv = (const u16*)d_in[1];
  const u16* wo = (const u16*)d_in[2];
  const u16* w1 = (const u16*)d_in[4];
  const u16* b1 = (const u16*)d_in[5];
  const u16* w2 = (const u16*)d_in[6];
  const u16* b2 = (const u16*)d_in[7];
  const u16* gate_w = (const u16*)d_in[8];
  char* ws = (char*)d_ws;
  k_proj<<<256, 256, 0, stream>>>(x, wqkv, w1, b1, w2, wo, gate_w, ws);
  k_attn<<<256, 768, 0, stream>>>(ws, ws, b2);
  k_out<<<256, 256, 0, stream>>>(ws, (u16*)d_out);
}
```

```cpp
#include <hip/hip_runtime.h>
#include <hip/hip_bf16.h>

namespace {

typedef __attribute__((ext_vector_type(8))) short bf16x8;
typedef __attribute__((ext_vector_type(4))) short s16x4;
typedef __attribute__((ext_vector_type(16))) float f32x16;
typedef __attribute__((ext_vector_type(4))) float f32x4;
typedef __attribute__((ext_vector_type(4))) unsigned u32x4;
typedef __attribute__((ext_vector_type(2))) __bf16 bf2_t;
typedef __attribute__((ext_vector_type(2))) float f2_t;
typedef unsigned short u16;

#define DI __device__ __forceinline__
#define MFMA32(a, b, c) __builtin_amdgcn_mfma_f32_32x32x16_bf16((a), (b), (c), 0, 0, 0)

constexpr int SEQ = 2048, DM = 512, FF = 1792, NH = 32;
constexpr float QSCALE = 0.25f * 1.4426950408889634f;
constexpr float RESCALE_THR = 4.0f;

constexpr size_t WS_QF = 0;
constexpr size_t WS_KF = WS_QF + (size_t)NH * 64 * 1024;
constexpr size_t WS_VF = WS_KF + (size_t)NH * 64 * 1024;
constexpr size_t WS_W2F = WS_VF + (size_t)NH * 64 * 1024;
constexpr size_t WS_WOF = WS_W2F + (size_t)16 * 112 * 1024;
constexpr size_t WS_HF = WS_WOF + (size_t)16 * 32 * 1024;
constexpr size_t WS_OF = WS_HF + (size_t)64 * 112 * 1024;
constexpr size_t WS_BB = WS_OF + (size_t)64 * 32 * 1024;
constexpr size_t WS_GATE = WS_BB + (size_t)64 * 16 * 2048;
constexpr size_t WS_END = WS_GATE + (size_t)SEQ * 4;

DI unsigned pk_bf16(float a, float b) {
  bf2_t r = __builtin_convertvector((f2_t){a, b}, bf2_t);
  return __builtin_bit_cast(unsigned, r);
}
DI float bf16lo(unsigned p) { return __builtin_bit_cast(float, p << 16); }
DI float bf16hi(unsigned p) { return __builtin_bit_cast(float, p & 0xffff0000u); }
DI float bf2f(u16 v) { return __builtin_bit_cast(float, (unsigned)v << 16); }
DI float rbf(float x) { return bf16lo(pk_bf16(x, 0.f)); }

DI u32x4 swap_to_frag(unsigned p0, unsigned p1, unsigned p2, unsigned p3) {
  auto r0 = __builtin_amdgcn_permlane32_swap(p0, p2, false, false);
  auto r1 = __builtin_amdgcn_permlane32_swap(p1, p3, false, false);
  u32x4 o;
  o[0] = r0[0]; o[1] = r1[0]; o[2] = r0[1]; o[3] = r1[1];
  return o;
}

DI void glds16(const void* g, void* l) {
  __builtin_amdgcn_global_load_lds((const __attribute__((address_space(1))) void*)(g),
                                   (__attribute__((address_space(3))) void*)(l), 16, 0, 0);
}
DI bf16x8 tr_frag(const char* pa, int pitch4) {
  const s16x4 lo = __builtin_amdgcn_ds_read_tr16_b64_v4i16((__attribute__((address_space(3))) s16x4*)(pa));
  const s16x4 hi = __builtin_amdgcn_ds_read_tr16_b64_v4i16((__attribute__((address_space(3))) s16x4*)(pa + pitch4));
  return __builtin_shufflevector(lo, hi, 0, 1, 2, 3, 4, 5, 6, 7);
}
#define STAMPN(ws, base, slot)
#define STAMP(ws, slot) STAMPN(ws, 0, slot)
#define STAMP2(ws, slot) STAMPN(ws, 65536, slot)
#define STAMP3(ws, slot) STAMPN(ws, 131072, slot)
#define WAIT_VM(n) asm volatile("s_waitcnt vmcnt(" #n ")" ::: "memory")
#define WAIT_LGKM0() asm volatile("s_waitcnt lgkmcnt(0)" ::: "memory")
#define BARRIER() __builtin_amdgcn_s_barrier()

constexpr int PJ_PITCH = 448;
constexpr int PJ_WCH = 64 * PJ_PITCH;
constexpr int PJ_XCH = 128 * 128;
constexpr int PJ_STAGE = PJ_WCH + PJ_XCH;
constexpr int PJ_RING = 3 * PJ_STAGE;
constexpr int PJ_SCRATCH = 5 * 1024;
constexpr int PJ_LDS = PJ_RING + 4 * PJ_SCRATCH;

template <bool QKV>
DI void proj_body(char* lds, const u16* __restrict__ x, const u16* __restrict__ W, const u16* __restrict__ b1,
                  const u16* __restrict__ w2, const u16* __restrict__ wo, const u16* __restrict__ gate_w,
                  char* ws, int mt, int c, int w, int lane) {
  constexpr int U = QKV ? 6 : 7;
  constexpr int LDW = QKV ? 3 * DM : FF;
  const int h = lane >> 5, r32 = lane & 31;
  const int row0 = mt * 128 + w * 32;
  char* scr = lds + PJ_RING + w * PJ_SCRATCH;
  const int wv = (mt * 8 + c) * 4 + w;
  const bool do_w2 = wv < 448;
  if constexpr (QKV) {
    const int rr = lane >> 2, cc = lane & 3;
#pragma unroll
    for (int i = 0; i < 4; ++i) {
      const int f = do_w2 ? 4 * wv + i : 0, ntile = f / 112, kstep = f - 112 * ntile;
      glds16(w2 + (size_t)(16 * kstep + rr) * DM + 32 * ntile + 8 * cc, scr + i * 1024 + lane * 16);
    }
    glds16(wo + (size_t)(16 * (wv & 31) + rr) * DM + 32 * (wv >> 5) + 8 * cc, scr + 4096 + lane * 16);
  } else {
    if (w == 0) glds16(b1 + 224 * (c - 8) + 8 * (lane < 28 ? lane : 0), lds + PJ_RING + lane * 16);
  }
  int soff[7];
#pragma unroll
  for (int t = 0; t < 7; ++t) {
    const int i = (4 * t + w) * 64 + lane;
    const int r = i / 28, cc = i - 28 * r;
    int col;
    if constexpr (QKV) {
      const int part = cc >> 3, wi = cc & 7;
      col = (part < 3 ? part * DM : 0) + 64 * c + 8 * wi;
    } else {
      col = 224 * (c - 8) + 8 * cc;
    }
    soff[t] = r * LDW + col;
  }
  const u16* xsrc = x + (size_t)(row0 + (lane >> 3)) * DM;
  const int xc0 = 8 * ((lane & 7) ^ ((lane >> 4) & 7)), xc1 = 8 * ((lane & 7) ^ ((4 + (lane >> 4)) & 7));
  auto issue = [&](int kc) {
    char* st = lds + (kc % 3) * PJ_STAGE;
    char* xd = st + PJ_WCH + w * 4096 + lane * 16;
#pragma unroll
    for (int t = 0; t < 4; ++t) glds16(xsrc + (size_t)(8 * t) * DM + 64 * kc + ((t & 1) ? xc1 : xc0), xd + t * 1024);
    const u16* src = W + (size_t)kc * 64 * LDW;
    char* dst = st + (w * 64 + lane) * 16;
#pragma unroll
    for (int t = 0; t < 7; ++t) glds16(src + soff[t], dst + t * 4096);
  };
  issue(0); issue(1); issue(2);
  const int q4 = (lane & 15) >> 2, p4 = lane & 3, blk = (lane >> 4) & 1;
  const int laneoff = (32 * h + q4) * PJ_PITCH + 32 * blk + 8 * p4;
  const int xoff = PJ_WCH + (w * 32 + r32) * 128;
  const int xsw = (r32 >> 1) & 7;
  f32x16 acc[U];
#pragma unroll
  for (int u = 0; u < U; ++u)
#pragma unroll
    for (int i = 0; i < 16; ++i) acc[u][i] = 0.f;
  struct Frags { bf16x8 wf[U]; bf16x8 xf; };
  auto rdw = [&](int kc, int ks, Frags& f) {
    const char* st = lds + (kc % 3) * PJ_STAGE;
    f.xf = *reinterpret_cast<const bf16x8*>(st + xoff + 16 * ((4 * h + ks) ^ xsw));
    const char* base = st + laneoff + ks * 8 * PJ_PITCH;
#pragma unroll
    for (int u = 0; u < U; ++u) f.wf[u] = tr_frag(base + 64 * u, 4 * PJ_PITCH);
  };
  auto mma = [&](const Frags& f) {
#pragma unroll
    for (int u = 0; u < U; ++u) {
      if (QKV && u >= 4) acc[u] = MFMA32(f.xf, f.wf[u], acc[u]);
      else acc[u] = MFMA32(f.wf[u], f.xf, acc[u]);
    }
  };
  Frags fA, fB;
  WAIT_VM(22);
  BARRIER();
  rdw(0, 0, fA);
#pragma unroll
  for (int kc = 0; kc < 8; ++kc) {
    rdw(kc, 1, fB);
    mma(fA);
    rdw(kc, 2, fA);
    mma(fB);
    rdw(kc, 3, fB);
    mma(fA);
    if (kc < 7) {
      if (kc <= 5) WAIT_VM(11); else WAIT_VM(0);
      WAIT_LGKM0();
      BARRIER();
      if (kc + 3 < 8) issue(kc + 3);
      rdw(kc + 1, 0, fA);
    }
    mma(fB);
  }
  const int mtile = row0 >> 5;
  if constexpr (QKV) {
    const bf16x8 gw = *reinterpret_cast<const bf16x8*>(gate_w + 8 * lane);
    bf16x8 gx[4];
#pragma unroll
    for (int r = 0; r < 4; ++r)
      gx[r] = *reinterpret_cast<const bf16x8*>(x + (size_t)(mt * 128 + 16 * c + 4 * w + r) * DM + 8 * lane);
    u16* qf = reinterpret_cast<u16*>(ws + WS_QF);
    u16* kf = reinterpret_cast<u16*>(ws + WS_KF);
    u16* vf = reinterpret_cast<u16*>(ws + WS_VF);
#pragma unroll
    for (int u = 0; u < 2; ++u) {
#pragma unroll
      for (int hh = 0; hh < 2; ++hh) {
        const int head = 4 * c + 2 * u + hh, o = 8 * hh;
        {
          const f32x16& a = acc[u];
          const u32x4 fr = swap_to_frag(pk_bf16(a[o + 0] * QSCALE, a[o + 1] * QSCALE),
                                        pk_bf16(a[o + 2] * QSCALE, a[o + 3] * QSCALE),
                                        pk_bf16(a[o + 4] * QSCALE, a[o + 5] * QSCALE),
                                        pk_bf16(a[o + 6] * QSCALE, a[o + 7] * QSCALE));
          *reinterpret_cast<u32x4*>(qf + ((size_t)(head * 64 + mtile) * 64 + lane) * 8) = fr;
        }
        {
          const f32x16& a = acc[2 + u];
          const u32x4 fr = swap_to_frag(pk_bf16(a[o + 0], a[o + 1]), pk_bf16(a[o + 2], a[o + 3]),
                                        pk_bf16(a[o + 4], a[o + 5]), pk_bf16(a[o + 6], a[o + 7]));
          *reinterpret_cast<u32x4*>(kf + ((size_t)(head * 64 + mtile) * 64 + lane) * 8) = fr;
        }
      }
      {
        const f32x16& a = acc[4 + u];
        const int head = 4 * c + 2 * u + (r32 >> 4), d = r32 & 15;
#pragma unroll
        for (int s = 0; s < 2; ++s) {
          u32x4 fr;
#pragma unroll
          for (int j = 0; j < 4; ++j) fr[j] = pk_bf16(a[8 * s + 2 * j], a[8 * s + 2 * j + 1]);
          *reinterpret_cast<u32x4*>(vf + ((((size_t)head * 64 + mtile) * 2 + s) * 32 + h * 16 + d) * 8) = fr;
        }
      }
    }
    const char* rb = scr + (8 * h + q4) * 64 + 32 * blk + 8 * p4;
#pragma unroll
    for (int i = 0; i < 5; ++i) {
      if (i == 4 || do_w2) {
        const size_t off = (i == 4) ? (WS_WOF + (size_t)wv * 1024) : (WS_W2F + (size_t)(4 * wv + i) * 1024);
        *reinterpret_cast<bf16x8*>(ws + off + lane * 16) = tr_frag(rb + i * 1024, 256);
      }
    }
    float* gate = reinterpret_cast<float*>(ws + WS_GATE);
#pragma unroll
    for (int r = 0; r < 4; ++r) {
      float sum = 0.f;
#pragma unroll
      for (int j = 0; j < 8; ++j) sum += rbf(bf2f((u16)gx[r][j]) * bf2f((u16)gw[j]));
#pragma unroll
      for (int o = 32; o >= 1; o >>= 1) sum += __shfl_xor(sum, o);
      if (lane == 0) gate[mt * 128 + 16 * c + 4 * w + r] = rbf(1.f / (1.f + __expf(-rbf(sum))));
    }
  } else {
    u16* hf = reinterpret_cast<u16*>(ws + WS_HF);
#pragma unroll
    for (int u = 0; u < U; ++u) {
      const int f0 = 224 * (c - 8) + 32 * u;
      unsigned pp[8];
#pragma unroll
      for (int g = 0; g < 4; ++g) {
        const s16x4 bv = *reinterpret_cast<const s16x4*>(lds + PJ_RING + 2 * (32 * u + 4 * h + 8 * g));
#pragma unroll
        for (int jj = 0; jj < 2; ++jj) {
          const int j = 2 * g + jj;
          const unsigned t0 = pk_bf16(acc[u][2 * j], acc[u][2 * j + 1]);
          const unsigned t = pk_bf16(bf16lo(t0) + bf2f((u16)bv[2 * jj]), bf16hi(t0) + bf2f((u16)bv[2 * jj + 1]));
          const float r0 = fmaxf(bf16lo(t), 0.f), r1 = fmaxf(bf16hi(t), 0.f);
          pp[j] = pk_bf16(r0 * r0, r1 * r1);
        }
      }
#pragma unroll
      for (int s = 0; s < 2; ++s) {
        const u32x4 fr = swap_to_frag(pp[4 * s + 0], pp[4 * s + 1], pp[4 * s + 2], pp[4 * s + 3]);
        *reinterpret_cast<u32x4*>(hf + ((size_t)(mtile * 112 + (f0 >> 4) + s) * 64 + lane) * 8) = fr;
      }
    }
  }
}

__global__ __launch_bounds__(256, 1) void k_proj(const u16* __restrict__ x, const u16* __restrict__ wqkv,
                                                 const u16* __restrict__ w1, const u16* __restrict__ b1,
                                                 const u16* __restrict__ w2, const u16* __restrict__ wo,
                                                 const u16* __restrict__ gate_w, char* ws) {
  __shared__ __attribute__((aligned(16))) char lds[PJ_LDS];
  const int tid = threadIdx.x, lane = tid & 63, w = tid >> 6;
  const int b = blockIdx.x, xcd = b & 7, idx = b >> 3, mt = idx & 15, c = xcd + 8 * (idx >> 4);
  if (c < 8) proj_body<true>(lds, x, wqkv, b1, w2, wo, gate_w, ws, mt, c, w, lane);
  else proj_body<false>(lds, x, w1, b1, w2, wo, gate_w, ws, mt, c, w, lane);
}

constexpr int AT_K = 0, AT_V = 65536, AT_ONES = 131072;
constexpr int AT_LDS = AT_ONES + 8192;

DI bf16x8 lds_b128(const char* p) { return *reinterpret_cast<const bf16x8*>(p); }

DI void soft_pv(const f32x16& S, const bf16x8 v0, const bf16x8 v1, f32x16& O) {
  u32x4 p0, p1;
#pragma unroll
  for (int j = 0; j < 4; ++j) {
    p0[j] = pk_bf16(__builtin_amdgcn_exp2f(S[2 * j]), __builtin_amdgcn_exp2f(S[2 * j + 1]));
    p1[j] = pk_bf16(__builtin_amdgcn_exp2f(S[8 + 2 * j]), __builtin_amdgcn_exp2f(S[8 + 2 * j + 1]));
  }
  O = MFMA32(v0, __builtin_bit_cast(bf16x8, p0), O);
  O = MFMA32(v1, __builtin_bit_cast(bf16x8, p1), O);
}

DI float lane_max16(const f32x16& S) {
  float m = fmaxf(S[0], S[1]);
#pragma unroll
  for (int i = 2; i < 16; i += 2) m = fmaxf(fmaxf(m, S[i]), S[i + 1]);
  return m;
}

DI void ffn2_role(const char* __restrict__ ws_in, char* ws_out, const u16* __restrict__ b2, int b, int w2,
                  int lane) {
  const int mt64 = (b & 7) * 4 + ((b >> 3) & 3), nt64 = b >> 5;
  const int mtile = 2 * mt64 + (w2 & 1), ntile = 2 * nt64 + (w2 >> 1), h = lane >> 5;
  const u16* hf = reinterpret_cast<const u16*>(ws_in + WS_HF) + (size_t)mtile * 112 * 512 + lane * 8;
  const u16* wf = reinterpret_cast<const u16*>(ws_in + WS_W2F) + (size_t)ntile * 112 * 512 + lane * 8;
  constexpr int PF = 8;
  bf16x8 ra[PF], rb[PF];
#pragma unroll
  for (int i = 0; i < PF; ++i) {
    ra[i] = *reinterpret_cast<const bf16x8*>(hf + (size_t)i * 512);
    rb[i] = *reinterpret_cast<const bf16x8*>(wf + (size_t)i * 512);
  }
  BARRIER();
  BARRIER();
  f32x16 acc = {};
#pragma unroll 1
  for (int k0 = 0; k0 < 112; k0 += PF) {
#pragma unroll
    for (int i = 0; i < PF; ++i) {
      acc = MFMA32(rb[i], ra[i], acc);
      const int kn = k0 + PF + i;
      if (kn < 112) {
        ra[i] = *reinterpret_cast<const bf16x8*>(hf + (size_t)kn * 512);
        rb[i] = *reinterpret_cast<const bf16x8*>(wf + (size_t)kn * 512);
      }
    }
  }
  unsigned pp[8];
#pragma unroll
  for (int gq = 0; gq < 4; ++gq) {
    const s16x4 bv = *reinterpret_cast<const s16x4*>(b2 + ntile * 32 + 8 * gq + 4 * h);
    pp[2 * gq] = pk_bf16(rbf(acc[4 * gq + 0]) + bf2f((u16)bv[0]), rbf(acc[4 * gq + 1]) + bf2f((u16)bv[1]));
    pp[2 * gq + 1] = pk_bf16(rbf(acc[4 * gq + 2]) + bf2f((u16)bv[2]), rbf(acc[4 * gq + 3]) + bf2f((u16)bv[3]));
  }
  u32x4* dst = reinterpret_cast<u32x4*>(ws_out + WS_BB) + ((size_t)(mtile * 16 + ntile) * 64 + lane) * 2;
  dst[0] = (u32x4){pp[0], pp[1], pp[2], pp[3]};
  dst[1] = (u32x4){pp[4], pp[5], pp[6], pp[7]};
}

__global__ __launch_bounds__(768, 3) void k_attn(const char* __restrict__ ws_in, char* ws_out,
                                                 const u16* __restrict__ b2) {
  __shared__ __attribute__((aligned(16))) char lds[AT_LDS];
  const int tid = threadIdx.x, lane = tid & 63, w = tid >> 6, h = lane >> 5, r32 = lane & 31;
  const int b = blockIdx.x;
  if (w >= 8) {
    ffn2_role(ws_in, ws_out, b2, b, w - 8, lane);
    return;
  }
  const int head = 4 * (b & 7) + ((b >> 3) & 3), qb = b >> 5;
  const int qtile = qb * 8 + w;
  const u16* qf = reinterpret_cast<const u16*>(ws_in + WS_QF);
  const u16* kf = reinterpret_cast<const u16*>(ws_in + WS_KF);
  const u16* vf = reinterpret_cast<const u16*>(ws_in + WS_VF);
  STAMP2(ws_out, 0);

  bf16x8 qv;
  {
    const u16* qp = qf + ((size_t)(head * 64 + qtile) * 64 + lane) * 8;
    asm volatile("global_load_dwordx4 %0, %1, off" : "=v"(qv) : "v"(qp) : "memory");
  }
  {
    const u16* ks = kf + (size_t)head * 64 * 512 + lane * 8;
    const u16* vs = vf + (size_t)head * 64 * 512 + lane * 8;
#pragma unroll
    for (int s = 0; s < 8; ++s) {
      const int ch = 8 * s + w;
      glds16(ks + (size_t)ch * 512, lds + AT_K + ch * 1024 + lane * 16);
      glds16(vs + (size_t)ch * 512, lds + AT_V + ch * 1024 + lane * 16);
    }
  }
  {
    u32x4 ones = {0x3f803f80u, 0x3f803f80u, 0x3f803f80u, 0x3f803f80u};
    *reinterpret_cast<u32x4*>(lds + AT_ONES + tid * 16) = ones;
  }
  asm volatile("s_waitcnt vmcnt(13)" : "+v"(qv)::"memory");
  WAIT_LGKM0();
  BARRIER();
  STAMP2(ws_out, 1);
  const char* kbase = lds + AT_K + lane * 16;
  const char* vbase = (r32 < 16) ? (lds + AT_V + (h * 16 + r32) * 16) : (lds + AT_ONES);
  const int vstep = (r32 < 16) ? 8192 : 0;

  float m = 0.f;
  f32x16 negm = {};
  f32x16 O = {};
  for (int g = 0; g < 8; ++g) {
    if (g == 1) { STAMP2(ws_out, 2); WAIT_VM(0); BARRIER(); STAMP2(ws_out, 3); }
    const char* kb = kbase + g * 8192;
    const char* vb = vbase + g * vstep;
#pragma unroll
    for (int u = 0; u < 8; ++u) {
      const int c = 8 * g + u;
      f32x16 S = MFMA32(lds_b128(kb + u * 1024), qv, negm);
      const bf16x8 v0 = lds_b128(vb + u * 1024), v1 = lds_b128(vb + u * 1024 + 512);
      const float mx = lane_max16(S);
      if (c == 0 || __any(mx > RESCALE_THR)) {
        const float cm = fmaxf(mx, __shfl_xor(mx, 32));
        const float mnew = m + (c == 0 ? cm : fmaxf(cm, 0.f));
        if (c > 0) {
          const float alpha = __builtin_amdgcn_exp2f(m - mnew);
#pragma unroll
          for (int i = 0; i < 16; ++i) O[i] *= alpha;
        }
        const float d = mnew - m;
#pragma unroll
        for (int i = 0; i < 16; ++i) { S[i] -= d; negm[i] = -mnew; }
        m = mnew;
      }
      soft_pv(S, v0, v1, O);
    }
  }
  STAMP2(ws_out, 4);
  {
    const float inv = 1.f / O[8];
    const u32x4 fr = swap_to_frag(pk_bf16(O[0] * inv, O[1] * inv), pk_bf16(O[2] * inv, O[3] * inv),
                                  pk_bf16(O[4] * inv, O[5] * inv), pk_bf16(O[6] * inv, O[7] * inv));
    u16* of = reinterpret_cast<u16*>(ws_out + WS_OF);
    *reinterpret_cast<u32x4*>(of + ((size_t)(qtile * 32 + head) * 64 + lane) * 8) = fr;
  }
  STAMP2(ws_out, 5);
}

__global__ __launch_bounds__(256, 1) void k_out(const char* __restrict__ ws, u16* __restrict__ out) {
  __shared__ __attribute__((aligned(16))) char lds[4 * 32 * 1024];
  const int tid = threadIdx.x, lane = tid & 63, w = tid >> 6, h = lane >> 5;
  const int b = blockIdx.x;
  const int mt64 = (b & 7) * 4 + ((b >> 3) & 3), nt64 = b >> 5;
  const int mtile = 2 * mt64 + (w & 1), ntile = 2 * nt64 + (w >> 1);
  {
    const char* src = (w < 2) ? (ws + WS_OF + (size_t)(2 * mt64 + w) * 32 * 1024)
                              : (ws + WS_WOF + (size_t)(2 * nt64 + (w - 2)) * 32 * 1024);
#pragma unroll
    for (int ks = 0; ks < 32; ++ks) glds16(src + ks * 1024 + lane * 16, lds + w * 32768 + ks * 1024 + lane * 16);
  }
  const u32x4* bsrc =
      reinterpret_cast<const u32x4*>(ws + WS_BB) + ((size_t)(mtile * 16 + ntile) * 64 + lane) * 2;
  const int m = mtile * 32 + (lane & 31);
  u32x4 b0, b1v;
  float g;
  {
    const float* gp = reinterpret_cast<const float*>(ws + WS_GATE) + m;
    asm volatile("global_load_dwordx4 %0, %1, off" : "=v"(b0) : "v"(bsrc) : "memory");
    asm volatile("global_load_dwordx4 %0, %1, off offset:16" : "=v"(b1v) : "v"(bsrc) : "memory");
    asm volatile("global_load_dword %0, %1, off" : "=v"(g) : "v"(gp) : "memory");
  }
  const char* ab = lds + (w & 1) * 32768 + lane * 16;
  const char* bb = lds + (2 + (w >> 1)) * 32768 + lane * 16;
  f32x16 aa = {};
#pragma unroll
  for (int st = 0; st < 4; ++st) {
    if (st == 0) WAIT_VM(27); else if (st == 1) WAIT_VM(19); else if (st == 2) WAIT_VM(11);
    else asm volatile("s_waitcnt vmcnt(0)" : "+v"(b0), "+v"(b1v), "+v"(g)::"memory");
    BARRIER();
#pragma unroll
    for (int k = 0; k < 8; ++k) {
      const int ks = 8 * st + k;
      aa = MFMA32(*reinterpret_cast<const bf16x8*>(bb + ks * 1024), *reinterpret_cast<const bf16x8*>(ab + ks * 1024),
                  aa);
    }
  }
  const float omg = rbf(1.f - g);
  unsigned pp[8];
#pragma unroll
  for (int j = 0; j < 8; ++j) {
    const unsigned bp = j < 4 ? b0[j] : b1v[j - 4];
    const float o0 = rbf(g * rbf(aa[2 * j])) + rbf(omg * bf16lo(bp));
    const float o1 = rbf(g * rbf(aa[2 * j + 1])) + rbf(omg * bf16hi(bp));
    pp[j] = pk_bf16(o0, o1);
  }
#pragma unroll
  for (int s = 0; s < 2; ++s) {
    const u32x4 fr = swap_to_frag(pp[4 * s + 0], pp[4 * s + 1], pp[4 * s + 2], pp[4 * s + 3]);
    *reinterpret_cast<u32x4*>(out + (size_t)m * DM + ntile * 32 + 16 * s + 8 * h) = fr;
  }
}

}

extern "C" void kernel_launch(void* const* d_in, const int* in_sizes, int n_in, void* d_out, int out_size,
                              void* d_ws, size_t ws_size, hipStream_t stream) {
  (void)in_sizes; (void)n_in; (void)out_size;
  if (ws_size < WS_END) return;
  const u16* x = (const u16*)d_in[0];
  const u16* wqkv = (const u16*)d_in[1];
  const u16* wo = (const u16*)d_in[2];
  const u16* w1 = (const u16*)d_in[4];
  const u16* b1 = (const u16*)d_in[5];
  const u16* w2 = (const u16*)d_in[6];
  const u16* b2 = (const u16*)d_in[7];
  const u16* gate_w = (const u16*)d_in[8];
  char* ws = (char*)d_ws;
  k_proj<<<256, 256, 0, stream>>>(x, wqkv, w1, b1, w2, wo, gate_w, ws);
  k_attn<<<256, 768, 0, stream>>>(ws, ws, b2);
  k_out<<<256, 256, 0, stream>>>(ws, (u16*)d_out);
}
```
